```python
import torch

def _softmax(x, axis=-1):
    return torch.softmax(x, dim=axis)
def _gelu(x):
    return torch.nn.functional.gelu(x, approximate="tanh")
def _silu(x):
    return torch.nn.functional.silu(x)
def _sigmoid(x):
    return torch.sigmoid(x)
def _layernorm(x, axis=-1, eps=1e-5):
    m = x.mean(dim=axis, keepdim=True)
    v = x.var(dim=axis, keepdim=True, unbiased=False)
    return (x - m) / torch.sqrt(v + eps)
def _scatter_add(tgt, idx, src, axis=0):
    return tgt.clone().index_add_(axis, idx, src)
def _segment_sum(src, seg, n):
    out = torch.zeros((n,) + src.shape[1:], dtype=src.dtype)
    return out.index_add_(0, seg, src)
def _pad(x, pw):
    flat = tuple(v for pair in reversed(pw) for v in pair)
    return torch.nn.functional.pad(x, flat)

kernel_name = 'interleave_layers__linear_kernel_hedgehog__dense_activation_squared_relu__prompt'

def setup_inputs():

    _w = (lambda a: (lambda t: t.to(torch.bfloat16) if t.is_floating_point() else t)(torch.as_tensor(a)))
    def _A_setup():
        B, S, D, H = 1, 2048, 448, 16
        Dh = D // H
        return {
            "x": _w((lambda *s: torch.randn(*s, dtype=torch.bfloat16) * 0.05)(B, S, D)),
            "wqkv": _w((lambda *s: torch.randn(*s, dtype=torch.bfloat16) * 0.2)(D, 3 * D)),
            "w_phi": _w((lambda *s: torch.randn(*s, dtype=torch.bfloat16) * 0.2)(H, Dh, Dh) * (Dh ** -0.5)),
            "wo": _w((lambda *s: torch.randn(*s, dtype=torch.bfloat16) * 0.05)(D, D)),
        }
    _A_in = _A_setup()

    def _B_setup():
        B, S, D, F = 1, 2048, 448, 1792
        return {
            "x": _w((lambda *s: torch.randn(*s, dtype=torch.bfloat16) * 0.05)(B, S, D)),
            "w1": _w((lambda *s: torch.randn(*s, dtype=torch.bfloat16) * 0.05)(D, F)),
            "b1": _w((lambda *s: torch.randn(*s, dtype=torch.bfloat16) * 0.2)(F)),
            "w2": _w((lambda *s: torch.randn(*s, dtype=torch.bfloat16) * 0.05)(F, D)),
            "b2": _w((lambda *s: torch.randn(*s, dtype=torch.bfloat16) * 0.4)(D)),
        }
    _B_in = _B_setup()

    return {
        "A_x": _A_in["x"],
        "A_wqkv": _A_in["wqkv"],
        "A_w_phi": _A_in["w_phi"],
        "A_wo": _A_in["wo"],
        "B_x": _B_in["x"],
        "B_w1": _B_in["w1"],
        "B_b1": _B_in["b1"],
        "B_w2": _B_in["w2"],
        "B_b2": _B_in["b2"],
    }

def reference(A_x, A_wqkv, A_w_phi, A_wo, B_x, B_w1, B_b1, B_w2, B_b2):
    def _A_ref(x, wqkv, w_phi, wo):
        B, S, D = x.shape
        H = 16; Dh = D // H
        qkv = (lambda x, *d: x.permute(*d))((x @ wqkv).reshape(B, S, 3, H, Dh), 2, 0, 3, 1, 4)
        q, k, v = qkv[0], qkv[1], qkv[2]
        pq = torch.einsum("bhsd,hdf->bhsf", q, w_phi)
        pk = torch.einsum("bhsd,hdf->bhsf", k, w_phi)
        phi_q = torch.concatenate([_softmax(pq, axis=-1), _softmax(-pq, axis=-1)], axis=-1)
        phi_k = torch.concatenate([_softmax(pk, axis=-1), _softmax(-pk, axis=-1)], axis=-1)
        kv = torch.einsum("bhsf,bhsd->bhfd", phi_k, v)
        z  = torch.einsum("bhsf->bhf", phi_k)
        num = torch.einsum("bhqf,bhfd->bhqd", phi_q, kv)
        den = torch.einsum("bhqf,bhf->bhq", phi_q, z)[..., None] + 1e-6
        o = num / den
        return (lambda x, *d: x.permute(*d))(o, 0, 2, 1, 3).reshape(B, S, D) @ wo

    def _B_ref(x, w1, b1, w2, b2):
        h = torch.einsum("bsd,df->bsf", x, w1) + b1
        zero = torch.zeros_like(h)
        r = torch.where(h > zero, h, zero)
        h = r * r
        out = torch.einsum("bsf,fd->bsd", h, w2) + b2
        return out

    x = A_x
    x = x + _A_ref(_layernorm(x, axis=-1), A_wqkv, A_w_phi, A_wo)
    x = x + _B_ref(_layernorm(x, axis=-1), B_w1, B_b1, B_w2, B_b2)
    x = x + _A_ref(_layernorm(x, axis=-1), A_wqkv, A_w_phi, A_wo)
    x = x + _B_ref(_layernorm(x, axis=-1), B_w1, B_b1, B_w2, B_b2)
    return x
```

```cpp
#include <hip/hip_runtime.h>
#include <hip/hip_bf16.h>
#include <stdint.h>

typedef __attribute__((ext_vector_type(8))) short bf16x8;
typedef __attribute__((ext_vector_type(4))) short bf16x4;
typedef __attribute__((ext_vector_type(4))) float f32x4;
typedef __attribute__((address_space(3))) bf16x4 lds_bf16x4;

constexpr int S_ = 2048, D_ = 448, H_ = 16, DH_ = 28, F_ = 1792;

__device__ __forceinline__ float bf2f(unsigned short u) { return __uint_as_float(((unsigned)u) << 16); }
__device__ __forceinline__ unsigned short f2bf(float f) {
  __hip_bfloat16 b = __float2bfloat16(f);
  return *reinterpret_cast<unsigned short*>(&b);
}
__device__ __forceinline__ bf16x4 tr_read(const short* p) {
  return __builtin_amdgcn_ds_read_tr16_b64_v4i16((lds_bf16x4*)(p));
}

template <int MT, int NT>
__device__ __forceinline__ void mma_chunk(const short* As, int lda, const short* Bs, int ldb, int kc,
                                          f32x4 (&acc)[MT][NT], int lane) {
  const int r16 = lane & 15, g = lane >> 4, q = (lane & 15) >> 2, p = lane & 3;
  for (int k0 = 0; k0 < kc; k0 += 32) {
    bf16x8 a[MT], b[NT];
#pragma unroll
    for (int mi = 0; mi < MT; ++mi) a[mi] = *(const bf16x8*)(As + (mi * 16 + r16) * lda + k0 + 8 * g);
#pragma unroll
    for (int ni = 0; ni < NT; ++ni) {
      bf16x4 lo = tr_read(Bs + (k0 + 8 * g + q) * ldb + ni * 16 + 4 * p);
      bf16x4 hi = tr_read(Bs + (k0 + 8 * g + 4 + q) * ldb + ni * 16 + 4 * p);
      b[ni] = __builtin_shufflevector(lo, hi, 0, 1, 2, 3, 4, 5, 6, 7);
    }
#pragma unroll
    for (int mi = 0; mi < MT; ++mi)
#pragma unroll
      for (int ni = 0; ni < NT; ++ni)
        acc[mi][ni] = __builtin_amdgcn_mfma_f32_16x16x32_bf16(a[mi], b[ni], acc[mi][ni], 0, 0, 0);
  }
}

template <int ROWS, int LDA>
__device__ __forceinline__ void ln_to_lds(const unsigned short* __restrict__ x, short* As, int tid) {
  const int sub = tid & 7;
  for (int r = tid >> 3; r < ROWS; r += 32) {
    bf16x8 v[7];
    float s = 0.f;
#pragma unroll
    for (int j = 0; j < 7; ++j) {
      v[j] = *(const bf16x8*)(x + r * D_ + (sub + 8 * j) * 8);
#pragma unroll
      for (int e = 0; e < 8; ++e) s += bf2f((unsigned short)v[j][e]);
    }
    s += __shfl_xor(s, 1); s += __shfl_xor(s, 2); s += __shfl_xor(s, 4);
    const float mean = s * (1.f / D_);
    float vs = 0.f;
#pragma unroll
    for (int j = 0; j < 7; ++j)
#pragma unroll
      for (int e = 0; e < 8; ++e) { float d = bf2f((unsigned short)v[j][e]) - mean; vs += d * d; }
    vs += __shfl_xor(vs, 1); vs += __shfl_xor(vs, 2); vs += __shfl_xor(vs, 4);
    const float rstd = rsqrtf(vs * (1.f / D_) + 1e-5f);
#pragma unroll
    for (int j = 0; j < 7; ++j) {
      bf16x8 o;
#pragma unroll
      for (int e = 0; e < 8; ++e) o[e] = (short)f2bf((bf2f((unsigned short)v[j][e]) - mean) * rstd);
      *(bf16x8*)(As + r * LDA + (sub + 8 * j) * 8) = o;
    }
  }
}

constexpr int LDA_ = 456;
constexpr int K1_LDB = 104;
__global__ __launch_bounds__(256) void k1_kernel(const unsigned short* __restrict__ x, const unsigned short* __restrict__ wqkv,
                                                 const unsigned short* __restrict__ wphi, unsigned short* __restrict__ phiq,
                                                 float* __restrict__ kvpart) {
  extern __shared__ __attribute__((aligned(16))) short smem[];
  short* As = smem;
  short* Bs = smem + 128 * LDA_;
  const int tid = threadIdx.x, lane = tid & 63, w = tid >> 6;
  const int rb = blockIdx.x, h = blockIdx.y;
  for (int i = tid; i < 2 * 64 * 3; i += 256) {
    int seg = i % 3, r = i / 3;
    *(bf16x4*)(Bs + r * K1_LDB + seg * 32 + 28) = (bf16x4){0, 0, 0, 0};
  }
  bf16x4 breg[6];
  auto bload = [&](int c) {
#pragma unroll
    for (int i = 0; i < 6; ++i) {
      int idx = tid + i * 256;
      if (idx < 1344) {
        int r = idx / 21, pc = idx % 21, seg = pc / 7, c4 = pc % 7;
        breg[i] = *(const bf16x4*)(wqkv + (size_t)(c * 64 + r) * 1344 + seg * 448 + h * 28 + c4 * 4);
      }
    }
  };
  auto bstore = [&](int buf) {
#pragma unroll
    for (int i = 0; i < 6; ++i) {
      int idx = tid + i * 256;
      if (idx < 1344) {
        int r = idx / 21, pc = idx % 21, seg = pc / 7, c4 = pc % 7;
        *(bf16x4*)(Bs + (buf * 64 + r) * K1_LDB + seg * 32 + c4 * 4) = breg[i];
      }
    }
  };
  bload(0);
  ln_to_lds<128, LDA_>(x + (size_t)rb * 128 * D_, As, tid);
  bstore(0);
  __syncthreads();
  f32x4 acc[2][6];
#pragma unroll
  for (int i = 0; i < 2; ++i)
#pragma unroll
    for (int j = 0; j < 6; ++j) acc[i][j] = (f32x4){0.f, 0.f, 0.f, 0.f};
  for (int c = 0; c < 7; ++c) {
    if (c + 1 < 7) bload(c + 1);
    mma_chunk<2, 6>(As + (w * 32) * LDA_ + c * 64, LDA_, Bs + (c & 1) * 64 * K1_LDB, K1_LDB, 64, acc, lane);
    if (c + 1 < 7) bstore((c + 1) & 1);
    __syncthreads();
  }
  short* QKV = smem;
  short* Wp = smem + 128 * K1_LDB;
  float* P = (float*)(smem + 128 * K1_LDB + 32 * 40);
  short* PHIK = smem + 128 * K1_LDB + 32 * 40 + 2 * 256 * 33;
#pragma unroll
  for (int mi = 0; mi < 2; ++mi)
#pragma unroll
    for (int ni = 0; ni < 6; ++ni)
#pragma unroll
      for (int r = 0; r < 4; ++r) {
        int row = w * 32 + mi * 16 + (lane >> 4) * 4 + r, col = ni * 16 + (lane & 15);
        float v = acc[mi][ni][r];
        if (col == 92) v = 1.f;
        QKV[row * K1_LDB + col] = (short)f2bf(v);
      }
  for (int i = tid; i < 32 * 32; i += 256) {
    int k = i >> 5, n = i & 31;
    Wp[k * 40 + n] = (k < 28 && n < 28) ? (short)wphi[h * 784 + k * 28 + n] : (short)0;
  }
  __syncthreads();
  {
    f32x4 pa[2][2], pb[2][2];
#pragma unroll
    for (int i = 0; i < 2; ++i)
#pragma unroll
      for (int j = 0; j < 2; ++j) { pa[i][j] = (f32x4){0.f, 0.f, 0.f, 0.f}; pb[i][j] = pa[i][j]; }
    mma_chunk<2, 2>(QKV + (w * 32) * K1_LDB, K1_LDB, Wp, 40, 32, pa, lane);
    mma_chunk<2, 2>(QKV + (w * 32) * K1_LDB + 32, K1_LDB, Wp, 40, 32, pb, lane);
#pragma unroll
    for (int mi = 0; mi < 2; ++mi)
#pragma unroll
      for (int ni = 0; ni < 2; ++ni)
#pragma unroll
        for (int r = 0; r < 4; ++r) {
          int row = w * 32 + mi * 16 + (lane >> 4) * 4 + r, col = ni * 16 + (lane & 15);
          P[row * 33 + col] = bf2f(f2bf(pa[mi][ni][r]));
          P[(128 + row) * 33 + col] = bf2f(f2bf(pb[mi][ni][r]));
        }
  }
  __syncthreads();
  {
    const float* pr = P + tid * 33;
    float mx = -INFINITY, mn = INFINITY;
    float v[28];
#pragma unroll
    for (int f = 0; f < 28; ++f) { v[f] = pr[f]; mx = fmaxf(mx, v[f]); mn = fminf(mn, v[f]); }
    float s1 = 0.f, s2 = 0.f;
    float e1[28], e2[28];
#pragma unroll
    for (int f = 0; f < 28; ++f) { e1[f] = __expf(v[f] - mx); e2[f] = __expf(mn - v[f]); s1 += e1[f]; s2 += e2[f]; }
    const float r1 = 1.f / s1, r2 = 1.f / s2;
    const int row = tid & 127;
    if (tid < 128) {
      unsigned short* dst = phiq + (size_t)(rb * 128 + row) * 896 + h * 56;
#pragma unroll
      for (int f = 0; f < 28; ++f) { dst[f] = f2bf(e1[f] * r1); dst[28 + f] = f2bf(e2[f] * r2); }
    } else {
      short* dst = PHIK + row * 72;
#pragma unroll
      for (int f = 0; f < 28; ++f) { dst[f] = (short)f2bf(e1[f] * r1); dst[28 + f] = (short)f2bf(e2[f] * r2); }
#pragma unroll
      for (int f = 56; f < 64; ++f) dst[f] = 0;
    }
  }
  __syncthreads();
  {
    const int g = lane >> 4, q = (lane & 15) >> 2, p = lane & 3;
    f32x4 kv[2] = {(f32x4){0.f, 0.f, 0.f, 0.f}, (f32x4){0.f, 0.f, 0.f, 0.f}};
#pragma unroll
    for (int k0 = 0; k0 < 128; k0 += 32) {
      bf16x4 alo = tr_read(PHIK + (k0 + 8 * g + q) * 72 + w * 16 + 4 * p);
      bf16x4 ahi = tr_read(PHIK + (k0 + 8 * g + 4 + q) * 72 + w * 16 + 4 * p);
      bf16x8 a = __builtin_shufflevector(alo, ahi, 0, 1, 2, 3, 4, 5, 6, 7);
#pragma unroll
      for (int ni = 0; ni < 2; ++ni) {
        bf16x4 lo = tr_read(QKV + (k0 + 8 * g + q) * K1_LDB + 64 + ni * 16 + 4 * p);
        bf16x4 hi = tr_read(QKV + (k0 + 8 * g + 4 + q) * K1_LDB + 64 + ni * 16 + 4 * p);
        bf16x8 b = __builtin_shufflevector(lo, hi, 0, 1, 2, 3, 4, 5, 6, 7);
        kv[ni] = __builtin_amdgcn_mfma_f32_16x16x32_bf16(a, b, kv[ni], 0, 0, 0);
      }
    }
    float* dst = kvpart + ((size_t)(rb * 16 + h)) * 64 * 32;
#pragma unroll
    for (int ni = 0; ni < 2; ++ni)
#pragma unroll
      for (int r = 0; r < 4; ++r) {
        int f = w * 16 + (lane >> 4) * 4 + r, d = ni * 16 + (lane & 15);
        dst[f * 32 + d] = kv[ni][r];
      }
  }
}

__global__ __launch_bounds__(256) void r_kernel(const float* __restrict__ kvpart, unsigned short* __restrict__ kvb) {
  int i = blockIdx.x * 256 + threadIdx.x;
  float s = 0.f;
#pragma unroll
  for (int rb = 0; rb < 16; ++rb) s += kvpart[(size_t)rb * 16 * 64 * 32 + i];
  int f = (i >> 5) & 63;
  kvb[i] = f < 56 ? f2bf(s) : (unsigned short)0;
}

__global__ __launch_bounds__(256) void k2_kernel(const unsigned short* __restrict__ x, const unsigned short* __restrict__ phiq,
                                                 const unsigned short* __restrict__ kvb, const unsigned short* __restrict__ wo,
                                                 unsigned short* __restrict__ xout) {
  extern __shared__ __attribute__((aligned(16))) short smem[];
  short* Os = smem;
  short* KV = smem + 64 * LDA_;
  const int tid = threadIdx.x, lane = tid & 63, w = tid >> 6;
  const int rb = blockIdx.x, cb = blockIdx.y;
  for (int i = tid; i < 16 * 64 * 4; i += 256) {
    int row = i >> 2, c = i & 3;
    *(bf16x8*)(KV + row * 40 + c * 8) = *(const bf16x8*)(kvb + row * 32 + c * 8);
  }
  __syncthreads();
  {
    const int r16 = lane & 15, g = lane >> 4, q = (lane & 15) >> 2, p = lane & 3;
    for (int hh = 0; hh < 4; ++hh) {
      const int h = w * 4 + hh;
      f32x4 nm[4][2];
#pragma unroll
      for (int i = 0; i < 4; ++i) { nm[i][0] = (f32x4){0.f, 0.f, 0.f, 0.f}; nm[i][1] = nm[i][0]; }
#pragma unroll
      for (int ks = 0; ks < 2; ++ks) {
        const int k0 = ks * 32;
        bf16x8 b[2];
#pragma unroll
        for (int ni = 0; ni < 2; ++ni) {
          bf16x4 lo = tr_read(KV + (h * 64 + k0 + 8 * g + q) * 40 + ni * 16 + 4 * p);
          bf16x4 hi = tr_read(KV + (h * 64 + k0 + 8 * g + 4 + q) * 40 + ni * 16 + 4 * p);
          b[ni] = __builtin_shufflevector(lo, hi, 0, 1, 2, 3, 4, 5, 6, 7);
        }
#pragma unroll
        for (int mi = 0; mi < 4; ++mi) {
          bf16x8 a = (bf16x8){0, 0, 0, 0, 0, 0, 0, 0};
          if (!(ks == 1 && g == 3)) a = *(const bf16x8*)(phiq + (size_t)(rb * 64 + mi * 16 + r16) * 896 + h * 56 + k0 + 8 * g);
#pragma unroll
          for (int ni = 0; ni < 2; ++ni) nm[mi][ni] = __builtin_amdgcn_mfma_f32_16x16x32_bf16(a, b[ni], nm[mi][ni], 0, 0, 0);
        }
      }
#pragma unroll
      for (int mi = 0; mi < 4; ++mi)
#pragma unroll
        for (int r = 0; r < 4; ++r) {
          float den = __shfl(nm[mi][1][r], (lane & 48) | 12);
          den = bf2f(f2bf(bf2f(f2bf(den)) + 1e-6f));
          const float inv = 1.f / den;
          const int row = mi * 16 + g * 4 + r;
          Os[row * LDA_ + h * 28 + r16] = (short)f2bf(bf2f(f2bf(nm[mi][0][r])) * inv);
          if (r16 < 12) Os[row * LDA_ + h * 28 + 16 + r16] = (short)f2bf(bf2f(f2bf(nm[mi][1][r])) * inv);
        }
    }
  }
  __syncthreads();
  short* Ws = KV;
  for (int i = tid; i < 448 * 8; i += 256) {
    int k = i >> 3, c = i & 7;
    *(bf16x8*)(Ws + k * 72 + c * 8) = *(const bf16x8*)(wo + (size_t)k * 448 + cb * 64 + c * 8);
  }
  __syncthreads();
  f32x4 acc[2][2];
#pragma unroll
  for (int i = 0; i < 2; ++i) { acc[i][0] = (f32x4){0.f, 0.f, 0.f, 0.f}; acc[i][1] = acc[i][0]; }
  const int wr = w >> 1, wc = w & 1;
  mma_chunk<2, 2>(Os + wr * 32 * LDA_, LDA_, Ws + wc * 32, 72, 448, acc, lane);
#pragma unroll
  for (int mi = 0; mi < 2; ++mi)
#pragma unroll
    for (int ni = 0; ni < 2; ++ni)
#pragma unroll
      for (int r = 0; r < 4; ++r) {
        int row = rb * 64 + wr * 32 + mi * 16 + (lane >> 4) * 4 + r, col = cb * 64 + wc * 32 + ni * 16 + (lane & 15);
        float o = bf2f(f2bf(acc[mi][ni][r]));
        xout[(size_t)row * D_ + col] = f2bf(bf2f(x[(size_t)row * D_ + col]) + o);
      }
}

__global__ __launch_bounds__(256) void k3_kernel(const unsigned short* __restrict__ x, const unsigned short* __restrict__ w1,
                                                 const unsigned short* __restrict__ b1, unsigned short* __restrict__ hbuf) {
  extern __shared__ __attribute__((aligned(16))) short smem[];
  short* As = smem;
  short* Bs = smem + 128 * LDA_;
  const int tid = threadIdx.x, lane = tid & 63, w = tid >> 6;
  const int rb = blockIdx.x, cb = blockIdx.y;
  bf16x8 breg[4];
  auto bload = [&](int c) {
#pragma unroll
    for (int i = 0; i < 4; ++i) {
      int idx = tid + i * 256, r = idx >> 4, c8 = idx & 15;
      breg[i] = *(const bf16x8*)(w1 + (size_t)(c * 64 + r) * F_ + cb * 128 + c8 * 8);
    }
  };
  auto bstore = [&](int buf) {
#pragma unroll
    for (int i = 0; i < 4; ++i) {
      int idx = tid + i * 256, r = idx >> 4, c8 = idx & 15;
      *(bf16x8*)(Bs + (buf * 64 + r) * 136 + c8 * 8) = breg[i];
    }
  };
  bload(0);
  ln_to_lds<128, LDA_>(x + (size_t)rb * 128 * D_, As, tid);
  bstore(0);
  __syncthreads();
  f32x4 acc[4][4];
#pragma unroll
  for (int i = 0; i < 4; ++i)
#pragma unroll
    for (int j = 0; j < 4; ++j) acc[i][j] = (f32x4){0.f, 0.f, 0.f, 0.f};
  const int wr = w >> 1, wc = w & 1;
  for (int c = 0; c < 7; ++c) {
    if (c + 1 < 7) bload(c + 1);
    mma_chunk<4, 4>(As + wr * 64 * LDA_ + c * 64, LDA_, Bs + (c & 1) * 64 * 136 + wc * 64, 136, 64, acc, lane);
    if (c + 1 < 7) bstore((c + 1) & 1);
    __syncthreads();
  }
#pragma unroll
  for (int mi = 0; mi < 4; ++mi)
#pragma unroll
    for (int ni = 0; ni < 4; ++ni) {
      const int col = cb * 128 + wc * 64 + ni * 16 + (lane & 15);
      const float bb = bf2f(b1[col]);
#pragma unroll
      for (int r = 0; r < 4; ++r) {
        int row = rb * 128 + wr * 64 + mi * 16 + (lane >> 4) * 4 + r;
        float t = bf2f(f2bf(bf2f(f2bf(acc[mi][ni][r])) + bb));
        t = fmaxf(t, 0.f);
        hbuf[(size_t)row * F_ + col] = f2bf(t * t);
      }
    }
}

__global__ __launch_bounds__(256) void k4_kernel(const unsigned short* __restrict__ x, const unsigned short* __restrict__ hbuf,
                                                 const unsigned short* __restrict__ w2, const unsigned short* __restrict__ b2,
                                                 unsigned short* __restrict__ xout) {
  __shared__ __attribute__((aligned(16))) short As[2][64][72];
  __shared__ __attribute__((aligned(16))) short Bs[2][64][72];
  const int tid = threadIdx.x, lane = tid & 63, w = tid >> 6;
  const int rb = blockIdx.x, cb = blockIdx.y;
  bf16x8 areg[2], breg[2];
  auto gload = [&](int c) {
#pragma unroll
    for (int i = 0; i < 2; ++i) {
      int idx = tid + i * 256, r = idx >> 3, c8 = idx & 7;
      areg[i] = *(const bf16x8*)(hbuf + (size_t)(rb * 64 + r) * F_ + c * 64 + c8 * 8);
      breg[i] = *(const bf16x8*)(w2 + (size_t)(c * 64 + r) * D_ + cb * 64 + c8 * 8);
    }
  };
  auto gstore = [&](int buf) {
#pragma unroll
    for (int i = 0; i < 2; ++i) {
      int idx = tid + i * 256, r = idx >> 3, c8 = idx & 7;
      *(bf16x8*)(&As[buf][r][c8 * 8]) = areg[i];
      *(bf16x8*)(&Bs[buf][r][c8 * 8]) = breg[i];
    }
  };
  gload(0);
  gstore(0);
  __syncthreads();
  f32x4 acc[2][2];
#pragma unroll
  for (int i = 0; i < 2; ++i) { acc[i][0] = (f32x4){0.f, 0.f, 0.f, 0.f}; acc[i][1] = acc[i][0]; }
  const int wr = w >> 1, wc = w & 1;
  for (int c = 0; c < 28; ++c) {
    if (c + 1 < 28) gload(c + 1);
    mma_chunk<2, 2>(&As[c & 1][wr * 32][0], 72, &Bs[c & 1][0][wc * 32], 72, 64, acc, lane);
    if (c + 1 < 28) gstore((c + 1) & 1);
    __syncthreads();
  }
#pragma unroll
  for (int mi = 0; mi < 2; ++mi)
#pragma unroll
    for (int ni = 0; ni < 2; ++ni) {
      const int col = cb * 64 + wc * 32 + ni * 16 + (lane & 15);
      const float bb = bf2f(b2[col]);
#pragma unroll
      for (int r = 0; r < 4; ++r) {
        int row = rb * 64 + wr * 32 + mi * 16 + (lane >> 4) * 4 + r;
        float o = bf2f(f2bf(bf2f(f2bf(acc[mi][ni][r])) + bb));
        xout[(size_t)row * D_ + col] = f2bf(bf2f(x[(size_t)row * D_ + col]) + o);
      }
    }
}

extern "C" void kernel_launch(void* const* d_in, const int* in_sizes, int n_in, void* d_out, int out_size, void* d_ws,
                              size_t ws_size, hipStream_t stream) {
  const unsigned short* A_x = (const unsigned short*)d_in[0];
  const unsigned short* wqkv = (const unsigned short*)d_in[1];
  const unsigned short* wphi = (const unsigned short*)d_in[2];
  const unsigned short* wo = (const unsigned short*)d_in[3];
  const unsigned short* w1 = (const unsigned short*)d_in[5];
  const unsigned short* b1 = (const unsigned short*)d_in[6];
  const unsigned short* w2 = (const unsigned short*)d_in[7];
  const unsigned short* b2 = (const unsigned short*)d_in[8];
  char* ws = (char*)d_ws;
  unsigned short* phiq = (unsigned short*)ws;
  float* kvpart = (float*)(ws + (4 << 20));
  unsigned short* kvb = (unsigned short*)(ws + (7 << 20));
  unsigned short* x1 = (unsigned short*)(ws + (8 << 20));
  unsigned short* x2 = (unsigned short*)(ws + (10 << 20));
  unsigned short* hbuf = (unsigned short*)(ws + (12 << 20));
  const size_t k1_lds = (128 * LDA_ + 2 * 64 * K1_LDB) * 2;
  const size_t k2_lds = (64 * LDA_ + 16 * 64 * 40) * 2;
  const size_t k3_lds = (128 * LDA_ + 2 * 64 * 136) * 2;
  (void)hipFuncSetAttribute((const void*)k1_kernel, hipFuncAttributeMaxDynamicSharedMemorySize, (int)k1_lds);
  (void)hipFuncSetAttribute((const void*)k2_kernel, hipFuncAttributeMaxDynamicSharedMemorySize, (int)k2_lds);
  (void)hipFuncSetAttribute((const void*)k3_kernel, hipFuncAttributeMaxDynamicSharedMemorySize, (int)k3_lds);
  const unsigned short* xin = A_x;
  for (int layer = 0; layer < 2; ++layer) {
    unsigned short* xo = layer == 0 ? x2 : (unsigned short*)d_out;
    k1_kernel<<<dim3(16, 16), 256, k1_lds, stream>>>(xin, wqkv, wphi, phiq, kvpart);
    r_kernel<<<128, 256, 0, stream>>>(kvpart, kvb);
    k2_kernel<<<dim3(32, 7), 256, k2_lds, stream>>>(xin, phiq, kvb, wo, x1);
    k3_kernel<<<dim3(16, 14), 256, k3_lds, stream>>>(x1, w1, b1, hbuf);
    k4_kernel<<<dim3(32, 7), 256, 0, stream>>>(x1, hbuf, w2, b2, xo);
    xin = x2;
  }
}
```

```cpp
#include <hip/hip_runtime.h>
#include <hip/hip_bf16.h>
#include <stdint.h>

typedef __attribute__((ext_vector_type(8))) short bf16x8;
typedef __attribute__((ext_vector_type(4))) short bf16x4;
typedef __attribute__((ext_vector_type(4))) float f32x4;
typedef __attribute__((address_space(3))) bf16x4 lds_bf16x4;

constexpr int S_ = 2048, D_ = 448, H_ = 16, DH_ = 28, F_ = 1792;
constexpr int LDA_ = 456;

__device__ __forceinline__ float bf2f(unsigned short u) { return __uint_as_float(((unsigned)u) << 16); }
__device__ __forceinline__ unsigned short f2bf(float f) {
  __hip_bfloat16 b = __float2bfloat16(f);
  return *reinterpret_cast<unsigned short*>(&b);
}
__device__ __forceinline__ bf16x4 tr_read(const short* p) {
  return __builtin_amdgcn_ds_read_tr16_b64_v4i16((lds_bf16x4*)(p));
}

template <int MT, int NT>
__device__ __forceinline__ void mma_chunk(const short* As, int lda, const short* Bs, int ldb, int kc,
                                          f32x4 (&acc)[MT][NT], int lane) {
  const int r16 = lane & 15, g = lane >> 4, q = (lane & 15) >> 2, p = lane & 3;
  for (int k0 = 0; k0 < kc; k0 += 32) {
    bf16x8 a[MT], b[NT];
#pragma unroll
    for (int mi = 0; mi < MT; ++mi) a[mi] = *(const bf16x8*)(As + (mi * 16 + r16) * lda + k0 + 8 * g);
#pragma unroll
    for (int ni = 0; ni < NT; ++ni) {
      bf16x4 lo = tr_read(Bs + (k0 + 8 * g + q) * ldb + ni * 16 + 4 * p);
      bf16x4 hi = tr_read(Bs + (k0 + 8 * g + 4 + q) * ldb + ni * 16 + 4 * p);
      b[ni] = __builtin_shufflevector(lo, hi, 0, 1, 2, 3, 4, 5, 6, 7);
    }
#pragma unroll
    for (int mi = 0; mi < MT; ++mi)
#pragma unroll
      for (int ni = 0; ni < NT; ++ni)
        acc[mi][ni] = __builtin_amdgcn_mfma_f32_16x16x32_bf16(a[mi], b[ni], acc[mi][ni], 0, 0, 0);
  }
}

__device__ __forceinline__ void ln_load(const unsigned short* __restrict__ x, bf16x8 (&v)[2][7], int tid) {
#pragma unroll
  for (int i = 0; i < 2; ++i)
#pragma unroll
    for (int j = 0; j < 7; ++j) v[i][j] = *(const bf16x8*)(x + ((tid >> 3) + 64 * i) * D_ + ((tid & 7) + 8 * j) * 8);
}
__device__ __forceinline__ void ln_store(bf16x8 (&v)[2][7], short* As, int tid) {
#pragma unroll
  for (int i = 0; i < 2; ++i) {
    float s = 0.f;
#pragma unroll
    for (int j = 0; j < 7; ++j)
#pragma unroll
      for (int e = 0; e < 8; ++e) s += bf2f((unsigned short)v[i][j][e]);
    s += __shfl_xor(s, 1); s += __shfl_xor(s, 2); s += __shfl_xor(s, 4);
    const float mean = s * (1.f / D_);
    float vs = 0.f;
#pragma unroll
    for (int j = 0; j < 7; ++j)
#pragma unroll
      for (int e = 0; e < 8; ++e) { float d = bf2f((unsigned short)v[i][j][e]) - mean; vs += d * d; }
    vs += __shfl_xor(vs, 1); vs += __shfl_xor(vs, 2); vs += __shfl_xor(vs, 4);
    const float rstd = rsqrtf(vs * (1.f / D_) + 1e-5f);
#pragma unroll
    for (int j = 0; j < 7; ++j) {
      bf16x8 o;
#pragma unroll
      for (int e = 0; e < 8; ++e) o[e] = (short)f2bf((bf2f((unsigned short)v[i][j][e]) - mean) * rstd);
      *(bf16x8*)(As + ((tid >> 3) + 64 * i) * LDA_ + ((tid & 7) + 8 * j) * 8) = o;
    }
  }
}

constexpr int K1_LDB = 104;
__global__ __launch_bounds__(512) void k1_kernel(const unsigned short* __restrict__ x, const unsigned short* __restrict__ wqkv,
                                                 const unsigned short* __restrict__ wphi, unsigned short* __restrict__ phiq,
                                                 float* __restrict__ kvpart) {
  extern __shared__ __attribute__((aligned(16))) short smem[];
  short* As = smem;
  short* Bs = smem + 128 * LDA_;
  const int tid = threadIdx.x, lane = tid & 63, w = tid >> 6;
  const int rb = blockIdx.x, h = blockIdx.y;
  bf16x8 av[2][7];
  ln_load(x + (size_t)rb * 128 * D_, av, tid);
  bf16x4 breg[7][3];
#pragma unroll
  for (int c = 0; c < 7; ++c)
#pragma unroll
    for (int i = 0; i < 3; ++i) {
      int idx = tid + i * 512;
      if (idx < 1344) {
        int r = idx / 21, pc = idx % 21, seg = pc / 7, c4 = pc % 7;
        breg[c][i] = *(const bf16x4*)(wqkv + (size_t)(c * 64 + r) * 1344 + seg * 448 + h * 28 + c4 * 4);
      }
    }
  auto bstore = [&](int c) {
#pragma unroll
    for (int i = 0; i < 3; ++i) {
      int idx = tid + i * 512;
      if (idx < 1344) {
        int r = idx / 21, pc = idx % 21, seg = pc / 7, c4 = pc % 7;
        *(bf16x4*)(Bs + ((c & 1) * 64 + r) * K1_LDB + seg * 32 + c4 * 4) = breg[c][i];
      }
    }
  };
  if (tid < 384) {
    int seg = tid % 3, r = tid / 3;
    *(bf16x4*)(Bs + r * K1_LDB + seg * 32 + 28) = (bf16x4){0, 0, 0, 0};
  }
  ln_store(av, As, tid);
  bstore(0); bstore(1);
  __syncthreads();
  f32x4 acc[1][6];
#pragma unroll
  for (int j = 0; j < 6; ++j) acc[0][j] = (f32x4){0.f, 0.f, 0.f, 0.f};
#pragma unroll
  for (int c = 0; c < 7; ++c) {
    mma_chunk<1, 6>(As + (w * 16) * LDA_ + c * 64, LDA_, Bs + (c & 1) * 64 * K1_LDB, K1_LDB, 64, acc, lane);
    __syncthreads();
    if (c + 2 < 7) bstore(c + 2);
  }
  __syncthreads();
  short* QKV = smem;
  short* Wp = smem + 128 * K1_LDB;
  float* P = (float*)(smem + 128 * K1_LDB + 32 * 40);
  short* PHIK = smem + 128 * K1_LDB + 32 * 40 + 2 * 256 * 33;
#pragma unroll
  for (int ni = 0; ni < 6; ++ni)
#pragma unroll
    for (int r = 0; r < 4; ++r) {
      int row = w * 16 + (lane >> 4) * 4 + r, col = ni * 16 + (lane & 15);
      float v = acc[0][ni][r];
      if (col == 92) v = 1.f;
      QKV[row * K1_LDB + col] = (short)f2bf(v);
    }
  for (int i = tid; i < 32 * 32; i += 512) {
    int k = i >> 5, n = i & 31;
    Wp[k * 40 + n] = (k < 28 && n < 28) ? (short)wphi[h * 784 + k * 28 + n] : (short)0;
  }
  __syncthreads();
  {
    f32x4 pa[1][2], pb[1][2];
#pragma unroll
    for (int j = 0; j < 2; ++j) { pa[0][j] = (f32x4){0.f, 0.f, 0.f, 0.f}; pb[0][j] = pa[0][j]; }
    mma_chunk<1, 2>(QKV + (w * 16) * K1_LDB, K1_LDB, Wp, 40, 32, pa, lane);
    mma_chunk<1, 2>(QKV + (w * 16) * K1_LDB + 32, K1_LDB, Wp, 40, 32, pb, lane);
#pragma unroll
    for (int ni = 0; ni < 2; ++ni)
#pragma unroll
      for (int r = 0; r < 4; ++r) {
        int row = w * 16 + (lane >> 4) * 4 + r, col = ni * 16 + (lane & 15);
        P[row * 33 + col] = bf2f(f2bf(pa[0][ni][r]));
        P[(128 + row) * 33 + col] = bf2f(f2bf(pb[0][ni][r]));
      }
  }
  __syncthreads();
  if (tid < 256) {
    const float* pr = P + tid * 33;
    float mx = -INFINITY, mn = INFINITY;
    float v[28];
#pragma unroll
    for (int f = 0; f < 28; ++f) { v[f] = pr[f]; mx = fmaxf(mx, v[f]); mn = fminf(mn, v[f]); }
    float s1 = 0.f, s2 = 0.f;
    float e1[28], e2[28];
#pragma unroll
    for (int f = 0; f < 28; ++f) { e1[f] = __expf(v[f] - mx); e2[f] = __expf(mn - v[f]); s1 += e1[f]; s2 += e2[f]; }
    const float r1 = 1.f / s1, r2 = 1.f / s2;
    const int row = tid & 127;
    if (tid < 128) {
      unsigned short* dst = phiq + (size_t)(rb * 128 + row) * 896 + h * 56;
#pragma unroll
      for (int f = 0; f < 28; ++f) { dst[f] = f2bf(e1[f] * r1); dst[28 + f] = f2bf(e2[f] * r2); }
    } else {
      short* dst = PHIK + row * 72;
#pragma unroll
      for (int f = 0; f < 28; ++f) { dst[f] = (short)f2bf(e1[f] * r1); dst[28 + f] = (short)f2bf(e2[f] * r2); }
#pragma unroll
      for (int f = 56; f < 64; ++f) dst[f] = 0;
    }
  }
  __syncthreads();
  {
    const int g = lane >> 4, q = (lane & 15) >> 2, p = lane & 3;
    const int ft = w & 3, dt = w >> 2;
    f32x4 kv = (f32x4){0.f, 0.f, 0.f, 0.f};
#pragma unroll
    for (int k0 = 0; k0 < 128; k0 += 32) {
      bf16x4 alo = tr_read(PHIK + (k0 + 8 * g + q) * 72 + ft * 16 + 4 * p);
      bf16x4 ahi = tr_read(PHIK + (k0 + 8 * g + 4 + q) * 72 + ft * 16 + 4 * p);
      bf16x8 a = __builtin_shufflevector(alo, ahi, 0, 1, 2, 3, 4, 5, 6, 7);
      bf16x4 lo = tr_read(QKV + (k0 + 8 * g + q) * K1_LDB + 64 + dt * 16 + 4 * p);
      bf16x4 hi = tr_read(QKV + (k0 + 8 * g + 4 + q) * K1_LDB + 64 + dt * 16 + 4 * p);
      bf16x8 b = __builtin_shufflevector(lo, hi, 0, 1, 2, 3, 4, 5, 6, 7);
      kv = __builtin_amdgcn_mfma_f32_16x16x32_bf16(a, b, kv, 0, 0, 0);
    }
    float* dst = kvpart + ((size_t)(rb * 16 + h)) * 64 * 32;
#pragma unroll
    for (int r = 0; r < 4; ++r) {
      int f = ft * 16 + (lane >> 4) * 4 + r, d = dt * 16 + (lane & 15);
      dst[f * 32 + d] = kv[r];
    }
  }
}

__global__ __launch_bounds__(256) void r_kernel(const float* __restrict__ kvpart, unsigned short* __restrict__ kvb) {
  int i = blockIdx.x * 256 + threadIdx.x;
  float s = 0.f;
#pragma unroll
  for (int rb = 0; rb < 16; ++rb) s += kvpart[(size_t)rb * 16 * 64 * 32 + i];
  int f = (i >> 5) & 63;
  kvb[i] = f < 56 ? f2bf(s) : (unsigned short)0;
}

__global__ __launch_bounds__(512) void k2_kernel(const unsigned short* __restrict__ x, const unsigned short* __restrict__ phiq,
                                                 const unsigned short* __restrict__ kvb, const unsigned short* __restrict__ wo,
                                                 unsigned short* __restrict__ xout) {
  extern __shared__ __attribute__((aligned(16))) short smem[];
  short* Os = smem;
  short* KV = smem + 64 * LDA_;
  const int tid = threadIdx.x, lane = tid & 63, w = tid >> 6;
  const int rb = blockIdx.x, cb = blockIdx.y;
  const int r16 = lane & 15, g = lane >> 4, q = (lane & 15) >> 2, p = lane & 3;
  bf16x8 kvreg[8];
#pragma unroll
  for (int i = 0; i < 8; ++i) { int idx = tid + i * 512; kvreg[i] = *(const bf16x8*)(kvb + (idx >> 2) * 32 + (idx & 3) * 8); }
  bf16x8 areg[2][4][2];
#pragma unroll
  for (int hh = 0; hh < 2; ++hh)
#pragma unroll
    for (int mi = 0; mi < 4; ++mi)
#pragma unroll
      for (int ks = 0; ks < 2; ++ks) {
        areg[hh][mi][ks] = (bf16x8){0, 0, 0, 0, 0, 0, 0, 0};
        if (!(ks == 1 && g == 3)) areg[hh][mi][ks] = *(const bf16x8*)(phiq + (size_t)(rb * 64 + mi * 16 + r16) * 896 + (w * 2 + hh) * 56 + ks * 32 + 8 * g);
      }
  bf16x8 wreg[7];
#pragma unroll
  for (int i = 0; i < 7; ++i) { int idx = tid + i * 512; wreg[i] = *(const bf16x8*)(wo + (size_t)(idx >> 3) * 448 + cb * 64 + (idx & 7) * 8); }
  unsigned short xres[4];
  const int wr = w >> 1, wc = w & 1;
#pragma unroll
  for (int i = 0; i < 8; ++i) { int idx = tid + i * 512; *(bf16x8*)(KV + (idx >> 2) * 40 + (idx & 3) * 8) = kvreg[i]; }
  __syncthreads();
#pragma unroll
  for (int hh = 0; hh < 2; ++hh) {
    const int h = w * 2 + hh;
    f32x4 nm[4][2];
#pragma unroll
    for (int i = 0; i < 4; ++i) { nm[i][0] = (f32x4){0.f, 0.f, 0.f, 0.f}; nm[i][1] = nm[i][0]; }
#pragma unroll
    for (int ks = 0; ks < 2; ++ks) {
      bf16x8 b[2];
#pragma unroll
      for (int ni = 0; ni < 2; ++ni) {
        bf16x4 lo = tr_read(KV + (h * 64 + ks * 32 + 8 * g + q) * 40 + ni * 16 + 4 * p);
        bf16x4 hi = tr_read(KV + (h * 64 + ks * 32 + 8 * g + 4 + q) * 40 + ni * 16 + 4 * p);
        b[ni] = __builtin_shufflevector(lo, hi, 0, 1, 2, 3, 4, 5, 6, 7);
      }
#pragma unroll
      for (int mi = 0; mi < 4; ++mi)
#pragma unroll
        for (int ni = 0; ni < 2; ++ni) nm[mi][ni] = __builtin_amdgcn_mfma_f32_16x16x32_bf16(areg[hh][mi][ks], b[ni], nm[mi][ni], 0, 0, 0);
    }
#pragma unroll
    for (int mi = 0; mi < 4; ++mi)
#pragma unroll
      for (int r = 0; r < 4; ++r) {
        float den = __shfl(nm[mi][1][r], (lane & 48) | 12);
        den = bf2f(f2bf(bf2f(f2bf(den)) + 1e-6f));
        const float inv = 1.f / den;
        const int row = mi * 16 + g * 4 + r;
        Os[row * LDA_ + h * 28 + r16] = (short)f2bf(bf2f(f2bf(nm[mi][0][r])) * inv);
        if (r16 < 12) Os[row * LDA_ + h * 28 + 16 + r16] = (short)f2bf(bf2f(f2bf(nm[mi][1][r])) * inv);
      }
  }
  __syncthreads();
  short* Ws = KV;
#pragma unroll
  for (int i = 0; i < 7; ++i) { int idx = tid + i * 512; *(bf16x8*)(Ws + (idx >> 3) * 72 + (idx & 7) * 8) = wreg[i]; }
#pragma unroll
  for (int ni = 0; ni < 2; ++ni)
#pragma unroll
    for (int r = 0; r < 4; ++r) { }
  __syncthreads();
  f32x4 acc[1][2];
  acc[0][0] = (f32x4){0.f, 0.f, 0.f, 0.f}; acc[0][1] = acc[0][0];
  mma_chunk<1, 2>(Os + wr * 16 * LDA_, LDA_, Ws + wc * 32, 72, 448, acc, lane);
#pragma unroll
  for (int ni = 0; ni < 2; ++ni)
#pragma unroll
    for (int r = 0; r < 4; ++r) {
      int row = rb * 64 + wr * 16 + (lane >> 4) * 4 + r, col = cb * 64 + wc * 32 + ni * 16 + (lane & 15);
      float o = bf2f(f2bf(acc[0][ni][r]));
      xout[(size_t)row * D_ + col] = f2bf(bf2f(x[(size_t)row * D_ + col]) + o);
    }
  (void)xres;
}

__global__ __launch_bounds__(512) void k3_kernel(const unsigned short* __restrict__ x, const unsigned short* __restrict__ w1,
                                                 const unsigned short* __restrict__ b1, unsigned short* __restrict__ hbuf) {
  extern __shared__ __attribute__((aligned(16))) short smem[];
  short* As = smem;
  short* Bs = smem + 128 * LDA_;
  const int tid = threadIdx.x, lane = tid & 63, w = tid >> 6;
  const int rb = blockIdx.x, cb = blockIdx.y;
  bf16x8 av[2][7];
  ln_load(x + (size_t)rb * 128 * D_, av, tid);
  bf16x8 breg[7][2];
#pragma unroll
  for (int c = 0; c < 7; ++c)
#pragma unroll
    for (int i = 0; i < 2; ++i) {
      int idx = tid + i * 512, r = idx >> 4, c8 = idx & 15;
      breg[c][i] = *(const bf16x8*)(w1 + (size_t)(c * 64 + r) * F_ + cb * 128 + c8 * 8);
    }
  auto bstore = [&](int c) {
#pragma unroll
    for (int i = 0; i < 2; ++i) {
      int idx = tid + i * 512, r = idx >> 4, c8 = idx & 15;
      *(bf16x8*)(Bs + ((c & 1) * 64 + r) * 136 + c8 * 8) = breg[c][i];
    }
  };
  ln_store(av, As, tid);
  bstore(0); bstore(1);
  __syncthreads();
  f32x4 acc[4][2];
#pragma unroll
  for (int i = 0; i < 4; ++i)
#pragma unroll
    for (int j = 0; j < 2; ++j) acc[i][j] = (f32x4){0.f, 0.f, 0.f, 0.f};
  const int wr = w >> 2, wc = w & 3;
#pragma unroll
  for (int c = 0; c < 7; ++c) {
    mma_chunk<4, 2>(As + wr * 64 * LDA_ + c * 64, LDA_, Bs + (c & 1) * 64 * 136 + wc * 32, 136, 64, acc, lane);
    if (c + 2 < 7) { __syncthreads(); bstore(c + 2); }
  }
#pragma unroll
  for (int mi = 0; mi < 4; ++mi)
#pragma unroll
    for (int ni = 0; ni < 2; ++ni) {
      const int col = cb * 128 + wc * 32 + ni * 16 + (lane & 15);
      const float bb = bf2f(b1[col]);
#pragma unroll
      for (int r = 0; r < 4; ++r) {
        int row = rb * 128 + wr * 64 + mi * 16 + (lane >> 4) * 4 + r;
        float t = bf2f(f2bf(bf2f(f2bf(acc[mi][ni][r])) + bb));
        t = fmaxf(t, 0.f);
        hbuf[(size_t)row * F_ + col] = f2bf(t * t);
      }
    }
}

__global__ __launch_bounds__(512) void k4_kernel(const unsigned short* __restrict__ x, const unsigned short* __restrict__ hbuf,
                                                 const unsigned short* __restrict__ w2, const unsigned short* __restrict__ b2,
                                                 unsigned short* __restrict__ xout) {
  __shared__ __attribute__((aligned(16))) short As[2][64][136];
  __shared__ __attribute__((aligned(16))) short Bs[2][128][72];
  const int tid = threadIdx.x, lane = tid & 63, w = tid >> 6;
  const int rb = blockIdx.x, cb = blockIdx.y;
  bf16x8 ar[3][2], br[3][2];
  auto gload = [&](int c, int s) {
#pragma unroll
    for (int i = 0; i < 2; ++i) {
      int idx = tid + i * 512;
      ar[s][i] = *(const bf16x8*)(hbuf + (size_t)(rb * 64 + (idx >> 4)) * F_ + c * 128 + (idx & 15) * 8);
      br[s][i] = *(const bf16x8*)(w2 + (size_t)(c * 128 + (idx >> 3)) * D_ + cb * 64 + (idx & 7) * 8);
    }
  };
  auto gstore = [&](int buf, int s) {
#pragma unroll
    for (int i = 0; i < 2; ++i) {
      int idx = tid + i * 512;
      *(bf16x8*)(&As[buf][idx >> 4][(idx & 15) * 8]) = ar[s][i];
      *(bf16x8*)(&Bs[buf][idx >> 3][(idx & 7) * 8]) = br[s][i];
    }
  };
  gload(0, 0); gload(1, 1); gload(2, 2);
  gstore(0, 0);
  __syncthreads();
  f32x4 acc[1][2];
  acc[0][0] = (f32x4){0.f, 0.f, 0.f, 0.f}; acc[0][1] = acc[0][0];
  const int wr = w >> 1, wc = w & 1;
#pragma unroll
  for (int c = 0; c < 14; ++c) {
    if (c + 1 < 14) gstore((c + 1) & 1, (c + 1) % 3);
    if (c + 3 < 14) gload(c + 3, c % 3);
    mma_chunk<1, 2>(&As[c & 1][wr * 16][0], 136, &Bs[c & 1][0][wc * 32], 72, 128, acc, lane);
    __syncthreads();
  }
#pragma unroll
  for (int ni = 0; ni < 2; ++ni) {
    const int col = cb * 64 + wc * 32 + ni * 16 + (lane & 15);
    const float bb = bf2f(b2[col]);
#pragma unroll
    for (int r = 0; r < 4; ++r) {
      int row = rb * 64 + wr * 16 + (lane >> 4) * 4 + r;
      float o = bf2f(f2bf(bf2f(f2bf(acc[0][ni][r])) + bb));
      xout[(size_t)row * D_ + col] = f2bf(bf2f(x[(size_t)row * D_ + col]) + o);
    }
  }
}

extern "C" void kernel_launch(void* const* d_in, const int* in_sizes, int n_in, void* d_out, int out_size, void* d_ws,
                              size_t ws_size, hipStream_t stream) {
  const unsigned short* A_x = (const unsigned short*)d_in[0];
  const unsigned short* wqkv = (const unsigned short*)d_in[1];
  const unsigned short* wphi = (const unsigned short*)d_in[2];
  const unsigned short* wo = (const unsigned short*)d_in[3];
  const unsigned short* w1 = (const unsigned short*)d_in[5];
  const unsigned short* b1 = (const unsigned short*)d_in[6];
  const unsigned short* w2 = (const unsigned short*)d_in[7];
  const unsigned short* b2 = (const unsigned short*)d_in[8];
  char* ws = (char*)d_ws;
  unsigned short* phiq = (unsigned short*)ws;
  float* kvpart = (float*)(ws + (4 << 20));
  unsigned short* kvb = (unsigned short*)(ws + (7 << 20));
  unsigned short* x1 = (unsigned short*)(ws + (8 << 20));
  unsigned short* x2 = (unsigned short*)(ws + (10 << 20));
  unsigned short* hbuf = (unsigned short*)(ws + (12 << 20));
  const size_t k1_lds = (128 * LDA_ + 2 * 64 * K1_LDB) * 2;
  const size_t k2_lds = (64 * LDA_ + 16 * 64 * 40) * 2;
  const size_t k3_lds = (128 * LDA_ + 2 * 64 * 136) * 2;
  (void)hipFuncSetAttribute((const void*)k1_kernel, hipFuncAttributeMaxDynamicSharedMemorySize, (int)k1_lds);
  (void)hipFuncSetAttribute((const void*)k2_kernel, hipFuncAttributeMaxDynamicSharedMemorySize, (int)k2_lds);
  (void)hipFuncSetAttribute((const void*)k3_kernel, hipFuncAttributeMaxDynamicSharedMemorySize, (int)k3_lds);
  const unsigned short* xin = A_x;
  for (int layer = 0; layer < 2; ++layer) {
    unsigned short* xo = layer == 0 ? x2 : (unsigned short*)d_out;
    k1_kernel<<<dim3(16, 16), 512, k1_lds, stream>>>(xin, wqkv, wphi, phiq, kvpart);
    r_kernel<<<128, 256, 0, stream>>>(kvpart, kvb);
    k2_kernel<<<dim3(32, 7), 512, k2_lds, stream>>>(xin, phiq, kvb, wo, x1);
    k3_kernel<<<dim3(16, 14), 512, k3_lds, stream>>>(x1, w1, b1, hbuf);
    k4_kernel<<<dim3(32, 7), 512, 0, stream>>>(x1, hbuf, w2, b2, xo);
    xin = x2;
  }
}
```
